```python
import torch

def _softmax(x, axis=-1):
    return torch.softmax(x, dim=axis)

def _gelu(x):
    return torch.nn.functional.gelu(x, approximate='tanh')

def _silu(x):
    return torch.nn.functional.silu(x)

def _sigmoid(x):
    return torch.sigmoid(x)

def _layernorm(x, axis=-1, eps=1e-05):
    m = x.mean(dim=axis, keepdim=True)
    v = x.var(dim=axis, keepdim=True, unbiased=False)
    return (x - m) / torch.sqrt(v + eps)

def _scatter_add(tgt, idx, src, axis=0):
    return tgt.clone().index_add_(axis, idx, src)

def _segment_sum(src, seg, n):
    out = torch.zeros((n,) + src.shape[1:], dtype=src.dtype)
    return out.index_add_(0, seg, src)

def _pad(x, pw):
    flat = tuple((v for pair in reversed(pw) for v in pair))
    return torch.nn.functional.pad(x, flat)
kernel_name = 'gather_scatter__tensor_product_equiv_mace__graph'

def setup_inputs():

    _w = lambda a: (lambda t: t.to(torch.bfloat16) if t.is_floating_point() else t)(torch.as_tensor(a))
    N, deg = (10000, 32)
    Lmax, C, De = (1, 128, 16)
    M = (Lmax + 1) ** 2
    E = N * deg
    src = (lambda lo, hi, *s: torch.randint(lo, hi, s))(0, N, E).to(torch.int64)
    dst = (lambda lo, hi, *s: torch.randint(lo, hi, s))(0, N, E).to(torch.int64)
    r = (lambda *s: torch.randn(*s, dtype=torch.bfloat16) * 0.05)(E, 3).to(float)
    r = r / (torch.linalg.norm(r, dim=-1, keepdim=True).to(float) + 1e-06)
    rx, ry, rz = (r[:, 0].clone(), r[:, 1].clone(), r[:, 2].clone())
    x2, y2, z2 = (rx * rx, ry * ry, rz * rz)
    sh_list = [torch.ones_like(rx), rx, ry, rz, rx * ry, ry * rz, 2.0 * z2 - x2 - y2, rx * rz, x2 - y2, ry * (3.0 * x2 - y2), rx * ry * rz, ry * (4.0 * z2 - x2 - y2), rz * (2.0 * z2 - 3.0 * x2 - 3.0 * y2), rx * (4.0 * z2 - x2 - y2), rz * (x2 - y2), rx * (x2 - 3.0 * y2)]
    sh16 = torch.stack(sh_list, dim=-1).to(float)
    pad = max(0, M - 16)
    sh_full = torch.cat([sh16, torch.zeros((E, pad), dtype=float)], dim=-1)[:, :M]
    cg = ((lambda *s: torch.randn(*s, dtype=torch.bfloat16) * 0.05)(M, M, M) * (1.0 / float(M))).to(float)
    return {'x': _w(((lambda *s: torch.randn(*s, dtype=torch.bfloat16) * 0.05)(N, M, C) * 0.3).to(float)), 'src': _w(src), 'dst': _w(dst), 'sh': _w(sh_full.to(float)), 'edge_attr': _w(((lambda *s: torch.randn(*s, dtype=torch.bfloat16) * 0.05)(E, De) * 0.3).to(float)), 'cg': _w(cg), 'w_r': _w(((lambda *s: torch.randn(*s, dtype=torch.bfloat16) * 0.05)(De, C) * (1.0 / float(De) ** 0.5)).to(float)), 'w_out': _w(((lambda *s: torch.randn(*s, dtype=torch.bfloat16) * 0.05)(C, C) * (1.0 / float(C) ** 0.5)).to(float))}

def reference(x, src, dst, sh, edge_attr, cg, w_r, w_out):
    N = x.shape[0]
    M = x.shape[1]
    C = x.shape[2]
    h = x[src]
    tp = torch.einsum('uvw,euc,ev->ewc', cg, h, sh)
    rw = edge_attr @ w_r
    msg = tp * rw[:, None, :]
    msg2 = msg.reshape(-1, M * C)
    agg = _segment_sum(msg2, dst, N).reshape(N, M, C)
    out = torch.einsum('nmc,cd->nmd', agg, w_out)
    return out
```

```cpp
#include <hip/hip_runtime.h>
#include <hip/hip_bf16.h>
#include <stdint.h>


typedef __attribute__((ext_vector_type(4))) short bf16x4_t;
typedef __attribute__((ext_vector_type(8))) short bf16x8_t;
typedef __attribute__((ext_vector_type(4))) float f32x4_t;

#define ELL_CAP 128

static constexpr int WAVES_PER_BLOCK = 8;
static constexpr int BLOCK_THREADS = WAVES_PER_BLOCK * 64;
static constexpr int WOT_STRIDE = 272;
static constexpr int RW_STRIDE = 528;
static constexpr int CHUNK = 8;
static constexpr int WAVE_LDS = CHUNK * RW_STRIDE;
static constexpr int LDS_WOT = 128 * WOT_STRIDE;
static constexpr int LDS_CG = 256;
static constexpr int LDS_TOTAL = LDS_WOT + LDS_CG + WAVES_PER_BLOCK * WAVE_LDS;

__device__ __forceinline__ float bf_lo(uint32_t v) { return __uint_as_float(v << 16); }
__device__ __forceinline__ float bf_hi(uint32_t v) { return __uint_as_float(v & 0xffff0000u); }

__device__ __forceinline__ uint32_t pack_bf16(float a, float b) {
    __hip_bfloat16 ha = __float2bfloat16(a);
    __hip_bfloat16 hb = __float2bfloat16(b);
    uint16_t ua = *reinterpret_cast<uint16_t*>(&ha);
    uint16_t ub = *reinterpret_cast<uint16_t*>(&hb);
    return (uint32_t)ua | ((uint32_t)ub << 16);
}

__global__ __launch_bounds__(256)
void build_ell_kernel(const int* __restrict__ dst, int E, int* __restrict__ cnt,
                      int* __restrict__ ell, int* __restrict__ ovf_list) {
    int e = blockIdx.x * blockDim.x + threadIdx.x;
    if (e >= E) return;
    int d = dst[e];
    int r = atomicAdd(&cnt[d + 1], 1);
    if (r < ELL_CAP) {
        ell[(size_t)d * ELL_CAP + r] = e;
    } else {
        int o = atomicAdd(&cnt[0], 1);
        ovf_list[o] = e;
    }
}

struct MainArgs {
    const uint16_t* x;
    const int* src;
    const int* dst;
    const uint16_t* sh;
    const uint16_t* ea;
    const uint16_t* cg;
    const uint16_t* w_r;
    const uint16_t* w_out;
    const int* cnt;
    const int* ell;
    const int* ovf_list;
    uint16_t* out;
    int N;
    int pad_;
};

__device__ __forceinline__ void process_chunk(const MainArgs& a, char* wlds, int lane,
                                              const bf16x4_t (&wr)[8],
                                              int eidv, int srcv, uint32_t shv0, uint32_t shv1,
                                              int base, int m, float (&B)[4][8]) {
    const int col = lane & 15;
    const int g = lane >> 4;
    int ecol = __shfl(eidv, base + (col < m ? col : 0));
    const uint2 eav = *reinterpret_cast<const uint2*>(a.ea + (size_t)ecol * 16 + 4 * g);
    bf16x4_t bfrag;
    bfrag[0] = (short)(eav.x & 0xffff); bfrag[1] = (short)(eav.x >> 16);
    bfrag[2] = (short)(eav.y & 0xffff); bfrag[3] = (short)(eav.y >> 16);
#pragma unroll
    for (int blk = 0; blk < 8; ++blk) {
        f32x4_t d = {0.f, 0.f, 0.f, 0.f};
        d = __builtin_amdgcn_mfma_f32_16x16x16bf16_1k(wr[blk], bfrag, d, 0, 0, 0);
        if (col < CHUNK) {
            int off = col * RW_STRIDE + (2 * blk + (g >> 1)) * 16 + (g & 1) * 256;
            *reinterpret_cast<f32x4_t*>(wlds + off) = d;
        }
    }
    uint4 xr[CHUNK];
#pragma unroll
    for (int t = 0; t < CHUNK; ++t) {
        if (t < m) {
            int s = __builtin_amdgcn_readlane(srcv, base + t);
            xr[t] = *reinterpret_cast<const uint4*>(a.x + (size_t)s * 512 + lane * 8);
        }
    }
    const int i = lane & 15;
#pragma unroll
    for (int t = 0; t < CHUNK; ++t) {
        if (t < m) {
            uint32_t s0 = (uint32_t)__builtin_amdgcn_readlane((int)shv0, base + t);
            uint32_t s1 = (uint32_t)__builtin_amdgcn_readlane((int)shv1, base + t);
            float shf[4] = {bf_lo(s0), bf_hi(s0), bf_lo(s1), bf_hi(s1)};
            f32x4_t r0 = *reinterpret_cast<const f32x4_t*>(wlds + t * RW_STRIDE + i * 16);
            f32x4_t r1 = *reinterpret_cast<const f32x4_t*>(wlds + t * RW_STRIDE + i * 16 + 256);
            float y[8];
            y[0] = bf_lo(xr[t].x) * r0[0]; y[1] = bf_hi(xr[t].x) * r0[1];
            y[2] = bf_lo(xr[t].y) * r0[2]; y[3] = bf_hi(xr[t].y) * r0[3];
            y[4] = bf_lo(xr[t].z) * r1[0]; y[5] = bf_hi(xr[t].z) * r1[1];
            y[6] = bf_lo(xr[t].w) * r1[2]; y[7] = bf_hi(xr[t].w) * r1[3];
#pragma unroll
            for (int v = 0; v < 4; ++v)
#pragma unroll
                for (int c = 0; c < 8; ++c) B[v][c] = fmaf(shf[v], y[c], B[v][c]);
        }
    }
}

__global__ __launch_bounds__(BLOCK_THREADS, 2)
void tp_main_kernel(MainArgs a) {
    __shared__ __attribute__((aligned(16))) char lds[LDS_TOTAL];
    const int tid = threadIdx.x;
    const int lane = tid & 63;
    const int wave = tid >> 6;
    const int col = lane & 15;
    const int g = lane >> 4;

    for (int idx = tid; idx < 128 * 128 / 2; idx += BLOCK_THREADS) {
        int c = idx >> 6;
        int d2 = (idx & 63) * 2;
        uint32_t v = *reinterpret_cast<const uint32_t*>(a.w_out + c * 128 + d2);
        *reinterpret_cast<uint16_t*>(lds + (d2) * WOT_STRIDE + c * 2) = (uint16_t)(v & 0xffff);
        *reinterpret_cast<uint16_t*>(lds + (d2 + 1) * WOT_STRIDE + c * 2) = (uint16_t)(v >> 16);
    }
    if (tid < 64) {
        reinterpret_cast<float*>(lds + LDS_WOT)[tid] = __uint_as_float((uint32_t)a.cg[tid] << 16);
    }
    bf16x4_t wr[8];
#pragma unroll
    for (int blk = 0; blk < 8; ++blk) {
#pragma unroll
        for (int j = 0; j < 4; ++j) wr[blk][j] = (short)a.w_r[(4 * g + j) * 128 + blk * 16 + col];
    }
    __syncthreads();

    char* wlds = lds + LDS_WOT + LDS_CG + wave * WAVE_LDS;
    const float* cgl = reinterpret_cast<const float*>(lds + LDS_WOT);
    const int gwave = blockIdx.x * WAVES_PER_BLOCK + wave;
    const int nwaves = gridDim.x * WAVES_PER_BLOCK;

    for (int n = a.N > 0 ? gwave : a.N; n < a.N; n += nwaves) {
        const int cntn = a.cnt[n + 1];
        const int cl = cntn < ELL_CAP ? cntn : ELL_CAP;
        float B[4][8];
#pragma unroll
        for (int v = 0; v < 4; ++v)
#pragma unroll
            for (int c = 0; c < 8; ++c) B[v][c] = 0.f;

        for (int sb = 0; sb < cl; sb += 64) {
            const int ms = (cl - sb) < 64 ? (cl - sb) : 64;
            int eidv = 0x7fffffff;
            if (lane < ms) eidv = a.ell[(size_t)n * ELL_CAP + sb + lane];
            {
                int rank = 0;
                for (int j = 0; j < ms; ++j) {
                    int o = __builtin_amdgcn_readlane(eidv, j);
                    rank += (o < eidv) ? 1 : 0;
                }
                if (lane >= ms) rank = lane;
                int* sl = reinterpret_cast<int*>(wlds);
                sl[rank] = eidv;
                eidv = sl[lane];
            }
            int srcv = 0;
            uint32_t shv0 = 0, shv1 = 0;
            if (lane < ms) {
                srcv = a.src[eidv];
                uint2 s = *reinterpret_cast<const uint2*>(a.sh + (size_t)eidv * 4);
                shv0 = s.x; shv1 = s.y;
            }
            for (int base = 0; base < ms; base += CHUNK) {
                int m = (ms - base) < CHUNK ? (ms - base) : CHUNK;
                process_chunk(a, wlds, lane, wr, eidv, srcv, shv0, shv1, base, m, B);
            }
        }
        if (cntn > ELL_CAP) {
            const int novf = a.cnt[0];
            for (int o = 0; o < novf; o += 64) {
                int e = -1;
                bool match = false;
                if (o + lane < novf) {
                    e = a.ovf_list[o + lane];
                    match = (a.dst[e] == n);
                }
                unsigned long long mask = __ballot(match);
                while (mask) {
                    int l = __builtin_ctzll(mask);
                    mask &= mask - 1;
                    int eq = __shfl(e, l);
                    int sq = a.src[eq];
                    uint2 s = *reinterpret_cast<const uint2*>(a.sh + (size_t)eq * 4);
                    process_chunk(a, wlds, lane, wr, eq, sq, s.x, s.y, 0, 1, B);
                }
            }
        }

        const int u = g;
        float p[4][8];
#pragma unroll
        for (int w = 0; w < 4; ++w)
#pragma unroll
            for (int c = 0; c < 8; ++c) {
                float s = 0.f;
#pragma unroll
                for (int v = 0; v < 4; ++v) s = fmaf(cgl[u * 16 + v * 4 + w], B[v][c], s);
                p[w][c] = s;
            }
        float q[2][8];
#pragma unroll
        for (int k = 0; k < 2; ++k)
#pragma unroll
            for (int c = 0; c < 8; ++c) {
                float keep = (u < 2) ? p[k][c] : p[2 + k][c];
                float send = (u < 2) ? p[2 + k][c] : p[k][c];
                q[k][c] = keep + __shfl_xor(send, 32);
            }
        float r[8];
#pragma unroll
        for (int c = 0; c < 8; ++c) {
            float keep = (u & 1) ? q[1][c] : q[0][c];
            float send = (u & 1) ? q[0][c] : q[1][c];
            r[c] = keep + __shfl_xor(send, 16);
        }
        {
            uint4 pk;
            pk.x = pack_bf16(r[0], r[1]); pk.y = pack_bf16(r[2], r[3]);
            pk.z = pack_bf16(r[4], r[5]); pk.w = pack_bf16(r[6], r[7]);
            *reinterpret_cast<uint4*>(wlds + u * WOT_STRIDE + (lane & 15) * 16) = pk;
        }
        bf16x8_t bfr[4];
#pragma unroll
        for (int ks = 0; ks < 4; ++ks) {
            uint4 t = make_uint4(0, 0, 0, 0);
            if (col < 4) t = *reinterpret_cast<const uint4*>(wlds + col * WOT_STRIDE + (32 * ks + 8 * g) * 2);
            bfr[ks] = __builtin_bit_cast(bf16x8_t, t);
        }
        char* olds = wlds + 4 * WOT_STRIDE;
#pragma unroll
        for (int blk = 0; blk < 8; ++blk) {
            f32x4_t acc = {0.f, 0.f, 0.f, 0.f};
#pragma unroll
            for (int ks = 0; ks < 4; ++ks) {
                uint4 t = *reinterpret_cast<const uint4*>(lds + (16 * blk + col) * WOT_STRIDE + (32 * ks + 8 * g) * 2);
                acc = __builtin_amdgcn_mfma_f32_16x16x32_bf16(__builtin_bit_cast(bf16x8_t, t), bfr[ks], acc, 0, 0, 0);
            }
            if (col < 4) {
                uint2 o;
                o.x = pack_bf16(acc[0], acc[1]); o.y = pack_bf16(acc[2], acc[3]);
                *reinterpret_cast<uint2*>(olds + col * 256 + (16 * blk + 4 * g) * 2) = o;
            }
        }
        uint4 ov = *reinterpret_cast<const uint4*>(olds + lane * 16);
        *reinterpret_cast<uint4*>(a.out + (size_t)n * 512 + lane * 8) = ov;
    }
}

extern "C" void kernel_launch(void* const* d_in, const int* in_sizes, int n_in,
                              void* d_out, int out_size, void* d_ws, size_t ws_size,
                              hipStream_t stream) {
    const int N = in_sizes[0] / 512;
    const int E = in_sizes[1];
    char* ws = (char*)d_ws;
    int* cnt = (int*)ws;
    size_t off = ((size_t)(N + 1) * 4 + 255) & ~(size_t)255;
    int* ell = (int*)(ws + off);
    off += ((size_t)N * ELL_CAP * 4 + 255) & ~(size_t)255;
    int* ovf = (int*)(ws + off);

    hipMemsetAsync(cnt, 0, (size_t)(N + 1) * 4, stream);
    build_ell_kernel<<<(E + 255) / 256, 256, 0, stream>>>((const int*)d_in[2], E, cnt, ell, ovf);

    MainArgs a;
    a.x = (const uint16_t*)d_in[0];
    a.src = (const int*)d_in[1];
    a.dst = (const int*)d_in[2];
    a.sh = (const uint16_t*)d_in[3];
    a.ea = (const uint16_t*)d_in[4];
    a.cg = (const uint16_t*)d_in[5];
    a.w_r = (const uint16_t*)d_in[6];
    a.w_out = (const uint16_t*)d_in[7];
    a.cnt = cnt;
    a.ell = ell;
    a.ovf_list = ovf;
    a.out = (uint16_t*)d_out;
    a.N = N;
    a.pad_ = 0;
    int nblocks = 512;
    tp_main_kernel<<<nblocks, BLOCK_THREADS, 0, stream>>>(a);
}
```

```cpp
#include <hip/hip_runtime.h>
#include <hip/hip_bf16.h>
#include <stdint.h>


typedef __attribute__((ext_vector_type(4))) short bf16x4_t;
typedef __attribute__((ext_vector_type(8))) short bf16x8_t;
typedef __attribute__((ext_vector_type(4))) float f32x4_t;
typedef __attribute__((ext_vector_type(2))) float f32x2_t;

static constexpr int NBUCKET = 512;
static constexpr int BCAP = 1024;
static constexpr int K1_THREADS = 512;
static constexpr int K1_PER_THREAD = 8;

static constexpr int WAVES_PER_BLOCK = 8;
static constexpr int BLOCK_THREADS = WAVES_PER_BLOCK * 64;
static constexpr int WOT_STRIDE = 272;
static constexpr int RW_STRIDE = 528;
static constexpr int WAVE_LDS = 8 * RW_STRIDE;
static constexpr int LDS_WOT = 128 * WOT_STRIDE;
static constexpr int LDS_CG = 256;
static constexpr int LDS_WR = 8 * 64 * 8;
static constexpr int LDS_LIST = BCAP * 8;
static constexpr int LDS_CNT = 64 * 4 * 2 + 64;
static constexpr int OFF_CG = LDS_WOT;
static constexpr int OFF_WR = OFF_CG + LDS_CG;
static constexpr int OFF_LIST = OFF_WR + LDS_WR;
static constexpr int OFF_CNT = OFF_LIST + LDS_LIST;
static constexpr int OFF_WAVE = OFF_CNT + LDS_CNT;
static constexpr int LDS_TOTAL = OFF_WAVE + WAVES_PER_BLOCK * WAVE_LDS;

__device__ __forceinline__ float bf_lo(uint32_t v) { return __uint_as_float(v << 16); }
__device__ __forceinline__ float bf_hi(uint32_t v) { return __uint_as_float(v & 0xffff0000u); }

__device__ __forceinline__ uint32_t pack_bf16(float a, float b) {
    __hip_bfloat16 ha = __float2bfloat16(a);
    __hip_bfloat16 hb = __float2bfloat16(b);
    uint16_t ua = *reinterpret_cast<uint16_t*>(&ha);
    uint16_t ub = *reinterpret_cast<uint16_t*>(&hb);
    return (uint32_t)ua | ((uint32_t)ub << 16);
}

static constexpr int EPB = K1_THREADS * K1_PER_THREAD;

__global__ __launch_bounds__(K1_THREADS)
void bucket_kernel(const int* __restrict__ dst, const int* __restrict__ src, int E, int npb,
                   int* __restrict__ cinfo, int2* __restrict__ seg) {
    __shared__ int hist[NBUCKET];
    __shared__ int wsum[K1_THREADS / 64];
    const int tid = threadIdx.x;
    const int lane = tid & 63;
    const int wv = tid >> 6;
    hist[tid] = 0;
    __syncthreads();
    const int e0 = blockIdx.x * EPB + tid;
    int dv[K1_PER_THREAD];
    int sv[K1_PER_THREAD];
    int rk[K1_PER_THREAD];
#pragma unroll
    for (int j = 0; j < K1_PER_THREAD; ++j) {
        int e = e0 + j * K1_THREADS;
        dv[j] = (e < E) ? dst[e] : -1;
        sv[j] = (e < E) ? src[e] : 0;
    }
    int bk[K1_PER_THREAD];
#pragma unroll
    for (int j = 0; j < K1_PER_THREAD; ++j) {
        rk[j] = 0; bk[j] = 0;
        if (dv[j] >= 0) {
            bk[j] = dv[j] / npb;
            rk[j] = atomicAdd(&hist[bk[j]], 1);
        }
    }
    __syncthreads();
    const int h = hist[tid];
    int incl = h;
#pragma unroll
    for (int d = 1; d < 64; d <<= 1) {
        int t = __shfl_up(incl, d);
        if (lane >= d) incl += t;
    }
    if (lane == 63) wsum[wv] = incl;
    __syncthreads();
    int wbase = 0;
    for (int i = 0; i < wv; ++i) wbase += wsum[i];
    const int off = wbase + incl - h;
    cinfo[blockIdx.x * NBUCKET + tid] = (off << 16) | h;
    __syncthreads();
    hist[tid] = off;
    __syncthreads();
    int2* segb = seg + (size_t)blockIdx.x * EPB;
#pragma unroll
    for (int j = 0; j < K1_PER_THREAD; ++j) {
        if (dv[j] >= 0) {
            int e = e0 + j * K1_THREADS;
            int dl = dv[j] - bk[j] * npb;
            segb[hist[bk[j]] + rk[j]] = make_int2(e | (dl << 26), sv[j]);
        }
    }
}

struct MainArgs {
    const uint16_t* x;
    const int* src;
    const int* dst;
    const uint16_t* sh;
    const uint16_t* ea;
    const uint16_t* cg;
    const uint16_t* w_r;
    const uint16_t* w_out;
    const int* cinfo;
    const int2* seg;
    uint16_t* out;
    int N;
    int npb;
    int G1;
    int pad_;
};

struct Acc {
    f32x2_t b[4][4];
};

__device__ __forceinline__ void rw_mfma(uint2 eav, char* wlds, const bf16x4_t* wrl, int lane) {
    const int col = lane & 15;
    const int g = lane >> 4;
    bf16x4_t bfrag;
    bfrag[0] = (short)(eav.x & 0xffff); bfrag[1] = (short)(eav.x >> 16);
    bfrag[2] = (short)(eav.y & 0xffff); bfrag[3] = (short)(eav.y >> 16);
    char* wbase = wlds + col * RW_STRIDE + (g >> 1) * 16 + (g & 1) * 256;
#pragma unroll
    for (int blk = 0; blk < 8; ++blk) {
        f32x4_t d = {0.f, 0.f, 0.f, 0.f};
        d = __builtin_amdgcn_mfma_f32_16x16x16bf16_1k(wrl[blk * 64 + lane], bfrag, d, 0, 0, 0);
        if (col < 8) *reinterpret_cast<f32x4_t*>(wbase + blk * 32) = d;
    }
}

__device__ __forceinline__ uint2 load_ea(const MainArgs& a, int eidv, int pos, int cnt, int lane) {
    int p = pos + (lane & 7);
    p = p < cnt ? p : 0;
    int e = __shfl(eidv, p);
    return *reinterpret_cast<const uint2*>(a.ea + (size_t)e * 16 + 4 * (lane >> 4));
}

__device__ __forceinline__ void load_rows(const MainArgs& a, int srcv, int pos, int cnt, int lane,
                                          uint4 (&xr)[4]) {
#pragma unroll
    for (int t = 0; t < 4; ++t) {
        int q = pos + t;
        q = q < cnt ? q : 0;
        int s = __builtin_amdgcn_readlane(srcv, q);
        xr[t] = *reinterpret_cast<const uint4*>(a.x + (size_t)s * 512 + lane * 8);
    }
}

__device__ __forceinline__ void compute_rows(const char* rwl, uint32_t shv0, uint32_t shv1, int pos,
                                             int cnt, const uint4 (&xr)[4], Acc& B) {
#pragma unroll
    for (int t = 0; t < 4; ++t) {
        if (pos + t < cnt) {
            uint32_t s0 = (uint32_t)__builtin_amdgcn_readlane((int)shv0, pos + t);
            uint32_t s1 = (uint32_t)__builtin_amdgcn_readlane((int)shv1, pos + t);
            const float sf0 = bf_lo(s0), sf1 = bf_hi(s0), sf2 = bf_lo(s1), sf3 = bf_hi(s1);
            f32x4_t r0 = *reinterpret_cast<const f32x4_t*>(rwl + t * RW_STRIDE);
            f32x4_t r1 = *reinterpret_cast<const f32x4_t*>(rwl + t * RW_STRIDE + 256);
            f32x2_t y[4];
            y[0] = (f32x2_t){bf_lo(xr[t].x), bf_hi(xr[t].x)} * (f32x2_t){r0[0], r0[1]};
            y[1] = (f32x2_t){bf_lo(xr[t].y), bf_hi(xr[t].y)} * (f32x2_t){r0[2], r0[3]};
            y[2] = (f32x2_t){bf_lo(xr[t].z), bf_hi(xr[t].z)} * (f32x2_t){r1[0], r1[1]};
            y[3] = (f32x2_t){bf_lo(xr[t].w), bf_hi(xr[t].w)} * (f32x2_t){r1[2], r1[3]};
#pragma unroll
            for (int j = 0; j < 4; ++j) {
                B.b[0][j] = __builtin_elementwise_fma((f32x2_t){sf0, sf0}, y[j], B.b[0][j]);
                B.b[1][j] = __builtin_elementwise_fma((f32x2_t){sf1, sf1}, y[j], B.b[1][j]);
                B.b[2][j] = __builtin_elementwise_fma((f32x2_t){sf2, sf2}, y[j], B.b[2][j]);
                B.b[3][j] = __builtin_elementwise_fma((f32x2_t){sf3, sf3}, y[j], B.b[3][j]);
            }
        }
    }
}

struct NodeMeta {
    int eidv;
    int srcv;
    uint32_t shv0, shv1;
    int cnt;
};

__device__ __forceinline__ void process_list(const MainArgs& a, char* wlds, const bf16x4_t* wrl,
                                             int lane, const NodeMeta& m, uint4 (&xa)[4], uint2 eav,
                                             Acc& B) {
    const char* rwl = wlds + (lane & 15) * 16;
    uint4 xb[4];
    const int cnt = m.cnt;
    for (int pos = 0; pos < cnt; pos += 8) {
        rw_mfma(eav, wlds, wrl, lane);
        eav = load_ea(a, m.eidv, pos + 8, cnt, lane);
        load_rows(a, m.srcv, pos + 4, cnt, lane, xb);
        compute_rows(rwl, m.shv0, m.shv1, pos, cnt, xa, B);
        load_rows(a, m.srcv, pos + 8, cnt, lane, xa);
        compute_rows(rwl + 4 * RW_STRIDE, m.shv0, m.shv1, pos + 4, cnt, xb, B);
    }
}

__device__ __forceinline__ NodeMeta load_meta(const MainArgs& a, const int2* llist, char* wlds,
                                              int lane, int off, int ms) {
    NodeMeta m;
    m.cnt = ms;
    int2 ent = make_int2(0x7fffffff, 0);
    if (lane < ms) ent = llist[off + lane];
    int rank = 0;
    for (int j = 0; j < ms; ++j) {
        int o = __builtin_amdgcn_readlane(ent.x, j);
        rank += (o < ent.x) ? 1 : 0;
    }
    if (lane >= ms) rank = lane;
    int2* sl = reinterpret_cast<int2*>(wlds);
    sl[rank] = ent;
    ent = sl[lane];
    m.eidv = ent.x & 0x03ffffff;
    m.srcv = ent.y;
    m.shv0 = 0; m.shv1 = 0;
    if (lane < ms) {
        uint2 sv = *reinterpret_cast<const uint2*>(a.sh + (size_t)m.eidv * 4);
        m.shv0 = sv.x; m.shv1 = sv.y;
    }
    return m;
}

__global__ __launch_bounds__(BLOCK_THREADS, 4)
void tp_main_kernel(MainArgs a) {
    __shared__ __attribute__((aligned(16))) char lds[LDS_TOTAL];
    const int tid = threadIdx.x;
    const int lane = tid & 63;
    const int wave = tid >> 6;
    const int col = lane & 15;
    const int g = lane >> 4;
    const int bkt = blockIdx.x;
    const int npb = a.npb;
    const int node0 = bkt * npb;
    const int G1 = a.G1;

    int* lcnt = reinterpret_cast<int*>(lds + OFF_CNT);
    int* loff = lcnt + 64;
    int* lmisc = lcnt + 128;
    int2* llist = reinterpret_cast<int2*>(lds + OFF_LIST);

    if (tid < 64) lcnt[tid] = 0;
    if (tid == 0) { lmisc[0] = WAVES_PER_BLOCK; lmisc[1] = 0; }
    __syncthreads();
    constexpr int SLOTS = 8;
    constexpr int PPP = BLOCK_THREADS / SLOTS;
    const int slot = tid & (SLOTS - 1);
    int tot = 0;
    for (int p0 = 0; p0 < G1; p0 += PPP) {
        int pc = p0 + (tid / SLOTS);
        int info = (pc < G1) ? a.cinfo[pc * NBUCKET + bkt] : 0;
        int pcnt = info & 0xffff;
        if (slot == 0) tot += pcnt;
    }
    {
        int t = tot;
#pragma unroll
        for (int d = 32; d > 0; d >>= 1) t += __shfl_xor(t, d);
        if (lane == 0) atomicAdd(&lmisc[1], t);
    }
    for (int idx = tid; idx < 128 * 128 / 2; idx += BLOCK_THREADS) {
        int c = idx >> 6;
        int d2 = (idx & 63) * 2;
        uint32_t v = *reinterpret_cast<const uint32_t*>(a.w_out + c * 128 + d2);
        *reinterpret_cast<uint16_t*>(lds + (d2) * WOT_STRIDE + c * 2) = (uint16_t)(v & 0xffff);
        *reinterpret_cast<uint16_t*>(lds + (d2 + 1) * WOT_STRIDE + c * 2) = (uint16_t)(v >> 16);
    }
    if (tid < 64) reinterpret_cast<float*>(lds + OFF_CG)[tid] = __uint_as_float((uint32_t)a.cg[tid] << 16);
    bf16x4_t* wrl = reinterpret_cast<bf16x4_t*>(lds + OFF_WR);
    {
        int blk = wave;
        bf16x4_t t;
#pragma unroll
        for (int j = 0; j < 4; ++j) t[j] = (short)a.w_r[(4 * g + j) * 128 + blk * 16 + col];
        wrl[blk * 64 + lane] = t;
    }
    __syncthreads();
    const int ctot = lmisc[1];
    const bool fits = ctot <= BCAP;
    if (fits) {
        for (int p0 = 0; p0 < G1; p0 += PPP) {
            int pc = p0 + (tid / SLOTS);
            int info = (pc < G1) ? a.cinfo[pc * NBUCKET + bkt] : 0;
            int pcnt = info & 0xffff;
            int poff = info >> 16;
            for (int k = slot; k < pcnt; k += SLOTS) {
                int2 ent = a.seg[(size_t)pc * EPB + poff + k];
                atomicAdd(&lcnt[(ent.x >> 26) & 63], 1);
            }
        }
        __syncthreads();
        if (tid < 64) {
            int c = lcnt[tid];
            int incl = c;
#pragma unroll
            for (int d = 1; d < 64; d <<= 1) {
                int t = __shfl_up(incl, d);
                if (lane >= d) incl += t;
            }
            loff[tid] = incl - c;
            lcnt[tid] = 0;
        }
        __syncthreads();
        for (int p0 = 0; p0 < G1; p0 += PPP) {
            int pc = p0 + (tid / SLOTS);
            int info = (pc < G1) ? a.cinfo[pc * NBUCKET + bkt] : 0;
            int pcnt = info & 0xffff;
            int poff = info >> 16;
            for (int k = slot; k < pcnt; k += SLOTS) {
                int2 ent = a.seg[(size_t)pc * EPB + poff + k];
                int dl = (ent.x >> 26) & 63;
                int r = atomicAdd(&lcnt[dl], 1);
                llist[loff[dl] + r] = ent;
            }
        }
        __syncthreads();
    }

    char* wlds = lds + OFF_WAVE + wave * WAVE_LDS;
    const float* cgl = reinterpret_cast<const float*>(lds + OFF_CG);
    const int u = g;
    int nl = wave;
    bool valid = (nl < npb) && (node0 + nl < a.N);
    NodeMeta meta;
    uint4 xa[4];
    uint2 eav = make_uint2(0, 0);
    meta.cnt = 0; meta.eidv = 0; meta.srcv = 0; meta.shv0 = 0; meta.shv1 = 0;
    if (valid && fits) {
        int c = lcnt[nl];
        meta = load_meta(a, llist, wlds, lane, loff[nl], c < 64 ? c : 64);
        eav = load_ea(a, meta.eidv, 0, meta.cnt, lane);
        load_rows(a, meta.srcv, 0, meta.cnt, lane, xa);
    }
    while (valid) {
        const int n = node0 + nl;
        Acc B;
#pragma unroll
        for (int v = 0; v < 4; ++v)
#pragma unroll
            for (int j = 0; j < 4; ++j) B.b[v][j] = (f32x2_t){0.f, 0.f};
        {
            const int cntn = fits ? lcnt[nl] : 0;
            const int offn = fits ? loff[nl] : 0;
            int sb = 64;
            int pc = 0, k0 = 0;
            unsigned long long mask = 0;
            int2 fent = make_int2(0, 0);
            while (true) {
                if (meta.cnt > 0) process_list(a, wlds, wrl, lane, meta, xa, eav, B);
                meta.cnt = 0;
                if (fits) {
                    if (sb >= cntn) break;
                    int ms = (cntn - sb) < 64 ? (cntn - sb) : 64;
                    meta = load_meta(a, llist, wlds, lane, offn + sb, ms);
                    sb += 64;
                } else {
                    while (mask == 0 && pc < G1) {
                        int info = a.cinfo[pc * NBUCKET + bkt];
                        int pcnt = info & 0xffff;
                        int poff = info >> 16;
                        bool match = false;
                        if (k0 + lane < pcnt) {
                            fent = a.seg[(size_t)pc * EPB + poff + k0 + lane];
                            match = (((fent.x >> 26) & 63) == nl);
                        }
                        mask = __ballot(match);
                        k0 += 64;
                        if (k0 >= pcnt) { k0 = 0; ++pc; }
                    }
                    if (mask == 0) break;
                    int l = __builtin_ctzll(mask);
                    mask &= mask - 1;
                    meta.eidv = __shfl(fent.x, l) & 0x03ffffff;
                    meta.srcv = __shfl(fent.y, l);
                    uint2 sv = *reinterpret_cast<const uint2*>(a.sh + (size_t)meta.eidv * 4);
                    meta.shv0 = sv.x; meta.shv1 = sv.y; meta.cnt = 1;
                }
                eav = load_ea(a, meta.eidv, 0, meta.cnt, lane);
                load_rows(a, meta.srcv, 0, meta.cnt, lane, xa);
            }
        }
        int nl_next;
        {
            int t = 0;
            if (lane == 0) t = atomicAdd(&lmisc[0], 1);
            nl_next = __builtin_amdgcn_readfirstlane(t);
        }
        const bool valid_next = (nl_next < npb) && (node0 + nl_next < a.N);
        int lane_e = lane;
        asm volatile("" : "+v"(lane_e));
        const int col_e = lane_e & 15;
        const int g_e = lane_e >> 4;
        float r[8];
#pragma unroll
        for (int c = 0; c < 8; ++c) {
            float pp[4];
#pragma unroll
            for (int w = 0; w < 4; ++w) {
                float sacc = 0.f;
#pragma unroll
                for (int v = 0; v < 4; ++v) sacc = fmaf(cgl[g_e * 16 + v * 4 + w], B.b[v][c >> 1][c & 1], sacc);
                pp[w] = sacc;
            }
            float q[2];
#pragma unroll
            for (int k = 0; k < 2; ++k) {
                auto sw = __builtin_amdgcn_permlane32_swap(__float_as_uint(pp[k]), __float_as_uint(pp[2 + k]), false, false);
                q[k] = __uint_as_float(sw[0]) + __uint_as_float(sw[1]);
            }
            auto sw2 = __builtin_amdgcn_permlane16_swap(__float_as_uint(q[0]), __float_as_uint(q[1]), false, false);
            r[c] = __uint_as_float(sw2[0]) + __uint_as_float(sw2[1]);
            if (c & 1) __builtin_amdgcn_sched_barrier(0);
        }
        uint4 pk;
        pk.x = pack_bf16(r[0], r[1]); pk.y = pack_bf16(r[2], r[3]);
        pk.z = pack_bf16(r[4], r[5]); pk.w = pack_bf16(r[6], r[7]);
        __builtin_amdgcn_sched_barrier(0);
        meta.cnt = 0;
        if (valid_next && fits) {
            int c = lcnt[nl_next];
            meta = load_meta(a, llist, wlds, lane, loff[nl_next], c < 64 ? c : 64);
            eav = load_ea(a, meta.eidv, 0, meta.cnt, lane);
            load_rows(a, meta.srcv, 0, meta.cnt, lane, xa);
        }
        __builtin_amdgcn_sched_barrier(0);
        *reinterpret_cast<uint4*>(wlds + g_e * WOT_STRIDE + col_e * 16) = pk;
        const char* bsrc = wlds + (col_e & 3) * WOT_STRIDE + 16 * g_e;
        char* olds = wlds + 4 * WOT_STRIDE;
#pragma unroll
        for (int blk = 0; blk < 8; ++blk) {
            f32x4_t acc = {0.f, 0.f, 0.f, 0.f};
#pragma unroll
            for (int ks = 0; ks < 4; ++ks) {
                uint4 t = *reinterpret_cast<const uint4*>(lds + (16 * blk + col_e) * WOT_STRIDE + (32 * ks + 8 * g_e) * 2);
                uint4 tb = *reinterpret_cast<const uint4*>(bsrc + 64 * ks);
                if (col_e >= 4) tb = make_uint4(0, 0, 0, 0);
                acc = __builtin_amdgcn_mfma_f32_16x16x32_bf16(__builtin_bit_cast(bf16x8_t, t), __builtin_bit_cast(bf16x8_t, tb), acc, 0, 0, 0);
            }
            if (col_e < 4) {
                uint2 o;
                o.x = pack_bf16(acc[0], acc[1]); o.y = pack_bf16(acc[2], acc[3]);
                *reinterpret_cast<uint2*>(olds + col_e * 256 + (16 * blk + 4 * g_e) * 2) = o;
            }
        }
        uint4 ov = *reinterpret_cast<const uint4*>(olds + lane_e * 16);
        *reinterpret_cast<uint4*>(a.out + (size_t)n * 512 + lane_e * 8) = ov;
        nl = nl_next;
        valid = valid_next;
    }
}

extern "C" void kernel_launch(void* const* d_in, const int* in_sizes, int n_in,
                              void* d_out, int out_size, void* d_ws, size_t ws_size,
                              hipStream_t stream) {
    const int N = in_sizes[0] / 512;
    const int E = in_sizes[1];
    const int npb = (N + NBUCKET - 1) / NBUCKET;
    const int G1 = (E + EPB - 1) / EPB;
    char* ws = (char*)d_ws;
    int* cinfo = (int*)ws;
    size_t off = ((size_t)G1 * NBUCKET * 4 + 255) & ~(size_t)255;
    int2* seg = (int2*)(ws + off);

    bucket_kernel<<<G1, K1_THREADS, 0, stream>>>((const int*)d_in[2], (const int*)d_in[1], E, npb, cinfo, seg);

    MainArgs a;
    a.x = (const uint16_t*)d_in[0];
    a.src = (const int*)d_in[1];
    a.dst = (const int*)d_in[2];
    a.sh = (const uint16_t*)d_in[3];
    a.ea = (const uint16_t*)d_in[4];
    a.cg = (const uint16_t*)d_in[5];
    a.w_r = (const uint16_t*)d_in[6];
    a.w_out = (const uint16_t*)d_in[7];
    a.cinfo = cinfo;
    a.seg = seg;
    a.out = (uint16_t*)d_out;
    a.N = N;
    a.npb = npb;
    a.G1 = G1;
    a.pad_ = 0;
    tp_main_kernel<<<NBUCKET, BLOCK_THREADS, 0, stream>>>(a);
}
```
